```python
import torch

def _softmax(x, axis=-1):
    return torch.softmax(x, dim=axis)
def _gelu(x):
    return torch.nn.functional.gelu(x, approximate="tanh")
def _silu(x):
    return torch.nn.functional.silu(x)
def _sigmoid(x):
    return torch.sigmoid(x)
def _layernorm(x, axis=-1, eps=1e-5):
    m = x.mean(dim=axis, keepdim=True)
    v = x.var(dim=axis, keepdim=True, unbiased=False)
    return (x - m) / torch.sqrt(v + eps)
def _scatter_add(tgt, idx, src, axis=0):
    return tgt.clone().index_add_(axis, idx, src)
def _segment_sum(src, seg, n):
    out = torch.zeros((n,) + src.shape[1:], dtype=src.dtype)
    return out.index_add_(0, seg, src)
def _pad(x, pw):
    flat = tuple(v for pair in reversed(pw) for v in pair)
    return torch.nn.functional.pad(x, flat)

kernel_name = 'parallel_sum__sliding_local_dilated__glu_gated_swiglu__prompt'

def setup_inputs():

    _w = (lambda a: (lambda t: t.to(torch.bfloat16) if t.is_floating_point() else t)(torch.as_tensor(a)))
    def _A_setup():
        B, S, D = 1, 2048, 640
        return {
            "x": _w((lambda *s: torch.randn(*s, dtype=torch.bfloat16) * 0.05)(B, S, D)),
            "wqkv": _w((lambda *s: torch.randn(*s, dtype=torch.bfloat16) * 0.05)(D, 3 * D)),
            "wo": _w((lambda *s: torch.randn(*s, dtype=torch.bfloat16) * 0.05)(D, D)),
        }
    _A_in = _A_setup()

    def _B_setup():
        B, S, D, F = 1, 2048, 640, 1728
        return {
            "x": _w((lambda *s: torch.randn(*s, dtype=torch.bfloat16) * 0.05)(B, S, D)),
            "w1": _w((lambda *s: torch.randn(*s, dtype=torch.bfloat16) * 0.8)(D, F) * (2.0 / D) ** 0.5),
            "w3": _w((lambda *s: torch.randn(*s, dtype=torch.bfloat16) * 0.8)(D, F) * (2.0 / D) ** 0.5),
            "w2": _w((lambda *s: torch.randn(*s, dtype=torch.bfloat16) * 0.8)(F, D) * (2.0 / F) ** 0.5),
        }
    _B_in = _B_setup()

    return {
        "A_x": _A_in["x"],
        "A_wqkv": _A_in["wqkv"],
        "A_wo": _A_in["wo"],
        "B_x": _B_in["x"],
        "B_w1": _B_in["w1"],
        "B_w3": _B_in["w3"],
        "B_w2": _B_in["w2"],
    }

def reference(A_x, A_wqkv, A_wo, B_x, B_w1, B_w3, B_w2):
    def _A_ref(x, wqkv, wo):
        B, S, D = x.shape
        H = 32; Dh = D // H
        W, dil = 8, 2
        qkv = (lambda x, *d: x.permute(*d))((x @ wqkv).reshape(B, S, 3, H, Dh), 2, 0, 3, 1, 4)
        q, k, v = qkv[0], qkv[1], qkv[2]
        scale = Dh ** -0.5
        pos = torch.arange(S)
        off = torch.arange(W) * dil
        idx = pos[:, None] - off[None, :]
        mask = idx >= 0
        idx_c = torch.where(mask, idx, 0).reshape(-1)
        kg = k[:, :, idx_c, :].reshape(B, H, S, W, Dh)
        vg = v[:, :, idx_c, :].reshape(B, H, S, W, Dh)
        logits = torch.einsum("bhsd,bhswd->bhsw", q, kg) * scale
        logits = torch.where(mask[None, None, :, :], logits, -1e9)
        attn = _softmax(logits, axis=-1)
        o = torch.einsum("bhsw,bhswd->bhsd", attn, vg)
        return (lambda x, *d: x.permute(*d))(o, 0, 2, 1, 3).reshape(B, S, D) @ wo

    def _B_ref(x, w1, w3, w2):
        gate = torch.einsum("bsd,df->bsf", x, w1)
        up = torch.einsum("bsd,df->bsf", x, w3)
        h = _silu(gate) * up
        out = torch.einsum("bsf,fd->bsd", h, w2)
        return out

    x = A_x
    xn = _layernorm(x, axis=-1)
    return x + _A_ref(xn, A_wqkv, A_wo) + _B_ref(xn, B_w1, B_w3, B_w2)
```

```cpp
#include <hip/hip_runtime.h>
#include <stdint.h>
#pragma clang diagnostic ignored "-Wint-to-pointer-cast"

typedef unsigned short u16;
typedef __attribute__((ext_vector_type(8))) __bf16 bf16x8;
typedef __attribute__((ext_vector_type(4))) short s16x4;
typedef __attribute__((ext_vector_type(2))) __bf16 bf16x2;
typedef __attribute__((ext_vector_type(2))) float f32x2;
typedef __attribute__((ext_vector_type(4))) float f32x4;
typedef __attribute__((ext_vector_type(4))) unsigned u32x4;
typedef __attribute__((ext_vector_type(2))) unsigned u32x2;
typedef __amdgpu_buffer_rsrc_t rsrc_t;
#define LAS __attribute__((address_space(3)))
#define DI __device__ __forceinline__

constexpr int S = 2048, D = 640, F = 1728;
constexpr int KS2 = 74;
constexpr unsigned O_HF = 0;
constexpr unsigned O_W2F = 32u * KS2 * 4096u;
constexpr unsigned O_ST = O_W2F + 8u * KS2 * 5120u;
constexpr unsigned O_CS = O_ST + 16384u;
constexpr unsigned O_SF = O_CS + 3u * 5376u * 4u;
constexpr unsigned O_EP = O_SF + 1024u;
constexpr unsigned O_DF = O_EP + 128u;
constexpr unsigned WS_BYTES = O_DF + 256u * 128u;
constexpr unsigned OOB = 0x40000000u;
constexpr int SC1 = 16;
constexpr unsigned SPIN_CAP = 1u << 16;

DI int rfl(int x) { return __builtin_amdgcn_readfirstlane(x); }
DI rsrc_t mk_rsrc(const void* p, unsigned bytes) { return __builtin_amdgcn_make_buffer_rsrc((void*)p, (short)0, (int)bytes, 0x00020000); }
DI void dma16(unsigned lds_addr, unsigned voff, rsrc_t srd, unsigned soff) {
  asm volatile("s_mov_b32 m0, %0\n\ts_nop 0\n\tbuffer_load_dwordx4 %1, %2, %3 offen lds" ::"s"(lds_addr), "v"(voff), "s"(srd), "s"(soff) : "memory");
}
DI void dma16c(unsigned lds_addr, unsigned voff, rsrc_t srd, unsigned soff) {
  asm volatile("s_mov_b32 m0, %0\n\ts_nop 0\n\tbuffer_load_dwordx4 %1, %2, %3 offen sc1 lds" ::"s"(lds_addr), "v"(voff), "s"(srd), "s"(soff) : "memory");
}
DI void dma16m(unsigned lds_addr, unsigned voff, rsrc_t srd, unsigned soff, unsigned long long mask) {
  asm volatile("s_mov_b32 m0, %0\n\ts_mov_b64 exec, %4\n\tbuffer_load_dwordx4 %1, %2, %3 offen lds\n\ts_mov_b64 exec, -1" ::"s"(lds_addr), "v"(voff), "s"(srd), "s"(soff), "s"(mask) : "memory");
}
#define WAIT_VM(n) asm volatile("s_waitcnt vmcnt(" #n ")" ::: "memory")
#define SB() __builtin_amdgcn_sched_barrier(0)
#define WAIT_LGKM0() do { asm volatile("" ::: "memory"); __builtin_amdgcn_s_waitcnt(0xC07F); asm volatile("" ::: "memory"); } while (0)
template <int N> DI void wait_vm() { asm volatile("s_waitcnt vmcnt(%0)" :: "i"(N) : "memory"); }
template <int OFF> DI void dma_more(unsigned voff, rsrc_t srd, unsigned soff) {
  asm volatile("buffer_load_dwordx4 %0, %1, %2 offen offset:%3 lds" ::"v"(voff), "s"(srd), "s"(soff), "i"(OFF) : "memory");
}
template <int OFF> DI void dma_morec(unsigned voff, rsrc_t srd, unsigned soff) {
  asm volatile("buffer_load_dwordx4 %0, %1, %2 offen offset:%3 sc1 lds" ::"v"(voff), "s"(srd), "s"(soff), "i"(OFF) : "memory");
}
#define BAR() do { asm volatile("" ::: "memory"); __builtin_amdgcn_s_barrier(); asm volatile("" ::: "memory"); } while (0)
DI f32x4 mfma(u32x4 a, u32x4 b, f32x4 c) {
  return __builtin_amdgcn_mfma_f32_16x16x32_bf16(__builtin_bit_cast(bf16x8, a), __builtin_bit_cast(bf16x8, b), c, 0, 0, 0);
}
template <bool FIRST> DI void mma(f32x4& c, u32x4 a, u32x4 b) {
  if (FIRST) asm volatile("v_mfma_f32_16x16x32_bf16 %0, %1, %2, 0" : "=&v"(c) : "v"(a), "v"(b));
  else asm volatile("v_mfma_f32_16x16x32_bf16 %0, %1, %2, %0" : "+v"(c) : "v"(a), "v"(b));
}
#define MMA_DRAIN() asm volatile("s_nop 7\n\ts_nop 3" ::: "memory")
DI u32x4 lds_r128(unsigned a) { return *(LAS u32x4*)a; }
DI u32x2 lds_tr(unsigned a) { return __builtin_bit_cast(u32x2, __builtin_amdgcn_ds_read_tr16_b64_v4i16((LAS s16x4*)a)); }
DI u32x4 lds_tr2(unsigned a, unsigned d) { u32x2 lo = lds_tr(a), hi = lds_tr(a + d); return u32x4{lo[0], lo[1], hi[0], hi[1]}; }
DI unsigned pk(float a, float b) { f32x2 v = {a, b}; return __builtin_bit_cast(unsigned, __builtin_convertvector(v, bf16x2)); }
DI u32x2 pk4(f32x4 v) { return u32x2{pk(v[0], v[1]), pk(v[2], v[3])}; }
DI float bf_lo(unsigned u) { return __uint_as_float(u << 16); }
DI float bf_hi(unsigned u) { return __uint_as_float(u & 0xffff0000u); }
DI void swap16(unsigned& a, unsigned& b) { auto r = __builtin_amdgcn_permlane16_swap(a, b, false, false); a = r[0]; b = r[1]; }
DI void swap32(unsigned& a, unsigned& b) { auto r = __builtin_amdgcn_permlane32_swap(a, b, false, false); a = r[0]; b = r[1]; }
DI float sum_swap16(float x, float y) { unsigned a = __float_as_uint(x), b = __float_as_uint(y); swap16(a, b); return __uint_as_float(a) + __uint_as_float(b); }
DI float sum_swap32(float x, float y) { unsigned a = __float_as_uint(x), b = __float_as_uint(y); swap32(a, b); return __uint_as_float(a) + __uint_as_float(b); }
DI float fmax_(float a, float b) { float inf = __builtin_inff(); asm("" : "+s"(inf)); return __builtin_amdgcn_fmed3f(a, b, inf); }
DI float fmax3_(float a, float b, float c) { float r; asm("v_max3_f32 %0, %1, %2, %3" : "=v"(r) : "v"(a), "v"(b), "v"(c)); return r; }
DI float red_max_g(float x) {
  unsigned a = __float_as_uint(x), b = a; swap16(a, b); x = fmax_(__uint_as_float(a), __uint_as_float(b));
  a = __float_as_uint(x); b = a; swap32(a, b); return fmax_(__uint_as_float(a), __uint_as_float(b));
}
template <int CTRL> DI float dpp_add(float x) {
  return x + __uint_as_float(__builtin_amdgcn_update_dpp(0u, __float_as_uint(x), CTRL, 0xf, 0xf, false));
}
DI float dot2(unsigned a, unsigned b, float c) { return __builtin_amdgcn_fdot2_f32_bf16(__builtin_bit_cast(bf16x2, a), __builtin_bit_cast(bf16x2, b), c, false); }
DI u32x4 ld128(rsrc_t r, unsigned vo, unsigned so) { return __builtin_amdgcn_raw_buffer_load_b128(r, vo, so, 0); }
DI u32x4 ld128c(rsrc_t r, unsigned vo, unsigned so) { return __builtin_amdgcn_raw_buffer_load_b128(r, vo, so, SC1); }
DI unsigned ld32c(rsrc_t r, unsigned vo, unsigned so) { return __builtin_amdgcn_raw_buffer_load_b32(r, vo, so, SC1); }
DI void ald128(u32x4& d, rsrc_t r, unsigned vo, unsigned so) { asm volatile("buffer_load_dwordx4 %0, %1, %2, %3 offen" : "=v"(d) : "v"(vo), "s"(r), "s"(so) : "memory"); }
DI void ald128c(u32x4& d, rsrc_t r, unsigned vo, unsigned so) { asm volatile("buffer_load_dwordx4 %0, %1, %2, %3 offen sc1" : "=v"(d) : "v"(vo), "s"(r), "s"(so) : "memory"); }
DI void ald32(unsigned& d, rsrc_t r, unsigned vo, unsigned so) { asm volatile("buffer_load_dword %0, %1, %2, %3 offen" : "=v"(d) : "v"(vo), "s"(r), "s"(so) : "memory"); }
DI void ald32c(unsigned& d, rsrc_t r, unsigned vo, unsigned so) { asm volatile("buffer_load_dword %0, %1, %2, %3 offen sc1" : "=v"(d) : "v"(vo), "s"(r), "s"(so) : "memory"); }
#define LANDED(x) asm volatile("" : "+v"(x))
DI void st128c(u32x4 v, rsrc_t r, unsigned vo, unsigned so) {
  asm volatile("buffer_store_dwordx4 %0, %1, %2, %3 offen sc1\n\ts_nop 1" :: "v"(v), "v"(vo), "s"(r), "s"(so) : "memory");
}
DI void st64c(u32x2 v, rsrc_t r, unsigned vo, unsigned so) { asm volatile("buffer_store_dwordx2 %0, %1, %2, %3 offen sc1" :: "v"(v), "v"(vo), "s"(r), "s"(so) : "memory"); }
DI void st32c(unsigned v, rsrc_t r, unsigned vo, unsigned so) { asm volatile("buffer_store_dword %0, %1, %2, %3 offen sc1" :: "v"(v), "v"(vo), "s"(r), "s"(so) : "memory"); }

#define STAMP(idx) do {} while (0)
#define STAMP_ARG
#define STAMP_PASS
#define STAMP_DUMP() do {} while (0)
#define STEP_DECL
#define STEP_STAMP(i) do {} while (0)
#define STEP_DUMP() do {} while (0)

template <int I> struct IC { static constexpr int v = I; };
template <int I, int N, class Fn> DI void static_for(Fn&& f) { if constexpr (I < N) { f(IC<I>{}); static_for<I + 1, N>(f); } }
DI int lane_id() { int x; asm volatile("v_mbcnt_lo_u32_b32 %0, -1, 0\n\tv_mbcnt_hi_u32_b32 %0, -1, %0" : "=v"(x)); return x; }
template <int C> DI unsigned xor_c(unsigned x) { if (C == 0) return x; unsigned r; asm volatile("v_xor_b32 %0, %2, %1" : "=v"(r) : "v"(x), "i"(C)); return r; }
DI unsigned xor_s(unsigned x, unsigned c) { unsigned r; asm volatile("v_xor_b32 %0, %2, %1" : "=v"(r) : "v"(x), "s"(c)); return r; }
DI unsigned add_s(unsigned x, unsigned c) { unsigned r; asm volatile("v_add_u32 %0, %2, %1" : "=v"(r) : "v"(x), "s"(c)); return r; }
DI int opq_v(int x) { asm volatile("" : "+v"(x)); return x; }
DI int opq_s(int x) { asm volatile("" : "+s"(x)); return x; }
#define P2_STAMP(i) do {} while (0)
struct P { const u16* x; const u16* wqkv; const u16* wo; const u16* w1; const u16* w3; const u16* w2; u16* out; char* ws; };

typedef const __attribute__((address_space(4))) P* kargp;
DI kargp kargs() { kargp q = (kargp)__builtin_amdgcn_kernarg_segment_ptr(); asm volatile("" : "+s"(q)); return q; }

constexpr unsigned L_EX = 0;
constexpr unsigned L_PART = 152 * 1024;
constexpr unsigned L_ST = 156 * 1024;
constexpr unsigned L_CS = L_ST + 2048;
constexpr unsigned L_RDY = L_CS + 1536;
constexpr unsigned LDS_BYTES = 160 * 1024;

DI unsigned w2_row(unsigned s, unsigned w) {
  const unsigned g = w >> 1, hf = w & 1, t = s - 20, p = (t * 171) >> 9, r3 = t - 3 * p;
  const unsigned ffn = 96 * p + (r3 < 2 ? 48 * r3 + 16 * hf + 4 * g : 48 * (g >> 1) + 32 + 8 * (g & 1) + 4 * hf);
  const unsigned att = s < 16 ? 40 * s + 20 * (g >> 1) + 8 * (g & 1) + 4 * hf : 160 * s - 2544 + 40 * g + 20 * hf;
  return s < 20 ? att : ffn;
}

#define PF0 0
#define PRO 2
#define HALF0 0
constexpr unsigned CODE_BYTES = 48 * 1024;
#define PRIO 3
#define NA0 2
#define NH0 4
#define HSL 2
#define OFIRST 0
DI int p2_f0(int i) { return 20 + 14 * i - (i == 3); }
constexpr bool seq_is_o(int j) { return OFIRST ? j < 5 : j >= 14; }
constexpr int seq_off(int j) { return OFIRST ? (j < 5 ? j : j - 5) : (j < 14 ? j : j - 14); }
template <bool ATT> DI unsigned bslot(int q, int idx) {
  if (!ATT) return q == 0 ? (idx < 6 ? 32768 + 5120 * idx : 136 * 1024 + 5120 * (idx - 6))
                 : q == 1 ? (idx < 4 ? 112 * 1024 + 5120 * idx : 65536 + 5120 * (idx - 4))
                          : (idx < 6 ? 5120 * idx : 88 * 1024 + 5120 * (idx - 6));
  return q == 0 ? 40960 + 5120 * idx : q == 1 ? 112 * 1024 + 5120 * idx : (idx < 6 ? 81920 + 5120 * idx : 25600 + 5120 * (idx - 6));
}
template <bool ATT> DI unsigned bslot_k(int k, int i) { return bslot<ATT>(k >> 1, 4 * (k & 1) + i); }
template <bool ATT, int q, int j> DI void bpiece(rsrc_t sws, unsigned src, int w, unsigned vo) {
  if (j == 0) dma16c(bslot<ATT>(q, w), vo, sws, src);
  else if (j < 4) dma_morec<(j & 3) * 1024>(vo, sws, src);
  else dma16c(bslot<ATT>(q, w) + 4096, vo, sws, src + 4096);
}

template <bool ATT, int NM>
struct Svc {
  static constexpr unsigned SLOT = ATT ? 128 * 1024 : 136 * 1024;
  static constexpr unsigned L_FE = ATT ? 152 * 1024 : 146 * 1024;
  static constexpr int OPT = 4;
  static constexpr int LW = ATT ? 8 : 12, FX = ATT ? 15 : 19;
  static constexpr int SP = ATT ? 14 : 13;
  rsrc_t sws, sx, sc, sj;
  unsigned jsrc[2], jdst[2], csrc, cld64, bsrc;
  unsigned w, idx, slab, part;
  unsigned vj, vc, aj, ac, vl16;
  u32x4 r[3], jr[2], xq; unsigned xd, ep; float sa, sb; f32x4 cacc;

  DI void setup(const P& p) {
    const unsigned b = blockIdx.x, l = vl16 >> 4, lr = l & 15, lg = l >> 4;
    idx = ATT ? (b & 7) * 14 + (b >> 3) - 18 : (b & 7) * 18 + (b >> 3);
    { const unsigned s = (b * 205) >> 10; sj = (!ATT && s < 20) ? mk_rsrc(p.wo, D * D * 2) : mk_rsrc(p.w2, F * D * 2); jsrc[0] = w2_row(s, w) * 1280 + (b - 5 * s) * 256; }
    if (ATT) { const unsigned u = 256 + idx, s = (u * 205) >> 10; jsrc[1] = w2_row(s, w) * 1280 + (u - 5 * s) * 256; }
    const bool big = ATT && idx < 84;
    const unsigned u = ATT ? idx + 60 : idx;
    slab = big ? idx : u >> 1; part = big ? 0 : 1 + (u & 1);
    const unsigned ld = slab < 30 ? 3840 : 3456;
    sc = mk_rsrc(slab < 30 ? p.wqkv : (slab < 57 ? p.w1 : p.w3), (ATT && idx >= 108) ? 0 : D * ld);
    csrc = (big ? 0 : 64 + 192 * part) * ld + (slab - (slab < 30 ? 0 : (slab < 57 ? 30 : 57))) * 128;
    cld64 = 64 * ld;
    vj = lg * 1280 + ((lr ^ ((lg | ((w & 2) << 1)) << 1)) << 4);
    vc = (8 * w + (l >> 3)) * ld + (((l & 7) ^ (((lg & 1) | ((w & 1) << 1)) << 1)) << 4);
    asm volatile("" : "+v"(vj), "+v"(vc), "+s"(jsrc[0]), "+s"(csrc) : "s"(sc), "s"(sj));
    if (ATT) asm volatile("" : "+s"(jsrc[1]));
  }
  DI unsigned job_dst(unsigned u, unsigned w_) { const unsigned s = (u * 205) >> 10, T = 8 * (u - 5 * s) + w_, cg = (T * 205) >> 10; return O_W2F + (cg * (KS2 * 5 - 5) + s * 5 + T) * 1024; }
  DI void chunk_mma(bool first, u32x4 f) {
    const u32x4 ones = {0x3f803f80u, 0x3f803f80u, 0x3f803f80u, 0x3f803f80u};
    if (first) cacc = mfma(ones, f, f32x4{0, 0, 0, 0}); else cacc = mfma(ones, f, cacc);
    asm volatile("" : "+v"(cacc));
  }
  template <int s, int m> DI void gap(unsigned r0, unsigned cg, unsigned cg2) {
    constexpr int k = NM - 1 - m;
    if constexpr (s >= 6 && s <= 9 && m == OPT) {
      if ((w & 3) == s - 6) st128c(jr[0], sws, vl16, jdst[0]);
      if (ATT) { if (((w + 2) & 3) == s - 6) st128c(jr[1], sws, vl16, jdst[1]); }
    }
    if constexpr (s == 17) { if constexpr (m % 3 == 0 && m >= 3 && m < 18) bpiece<ATT, 0, m / 3 - 1>(sws, bsrc, w, vl16); }
    if constexpr (s == 18) { if constexpr (m % 3 == 1 && m < 15) bpiece<ATT, 1, m / 3>(sws, bsrc + 10240, w, vl16); }
    if constexpr (s == 2) {
      if (k == 4) dma16(SLOT + w * 1024, vj, sj, jsrc[0]);
      if (k == 2 && ATT) dma16(SLOT + 8192 + w * 1024, vj, sj, jsrc[1]);
      if (k == 0) dma16(SLOT + (ATT ? 16384 : 8192) + w * 1024, vc, sc, csrc);
    } else if constexpr (s == 3) {
      if (m == OPT) { if (w == 7) ald32c(ep, sws, 0, O_EP); }
      if (k == 3) ald128(xq, sx, vl16, (blockIdx.x * 8 + w) * 1280);
      if (k == 1) ald32(xd, sx, vl16 >> 2, (blockIdx.x * 8 + w) * 1280 + 1024);
    } else if constexpr (s == 4) {
      if (k == 3) {
        const unsigned l = lane_id(), lg = l >> 4, q = (l >> 2) & 3, t = (8 * lg + q) * 128 + (l & 3) * 8, ww = opq_s(w);
        aj = SLOT + 2 * t - (l & 3) * 8 + ((ww ^ (q | ((lg & 1) << 2))) << 5);
        ac = SLOT + (ww >> 2) * 4096 + t + (((ww & 3) ^ ((q >> 1) | ((lg & 1) << 1))) << 5);
        asm volatile("" : "+v"(aj), "+v"(ac));
      }
      if (k == 1) { jdst[0] = job_dst(opq_s(blockIdx.x), opq_s(w)); asm volatile("" : "+s"(jdst[0])); }
    } else if constexpr (s == 5) {
      if (m == 3 && ATT) { jdst[1] = job_dst(256 + opq_s(idx), opq_s(w)); asm volatile("" : "+s"(jdst[1])); }
      if (k == 7) r[2] = lds_tr2(ac + (ATT ? 16384 : 8192), 512);
      if (k == 3) jr[0] = lds_tr2(aj, 1024);
      if (k == 1 && ATT) jr[1] = lds_tr2(aj + 8192, 1024);
      if (k == 0) chunk_mma(true, r[2]);
    } else if constexpr (s == 6) {
      if (m == 1) {
        LANDED(xq); LANDED(xd);
        sa = dot2(xd, 0x3f803f80u, 0.f); sb = dot2(xd, xd, 0.f);
#pragma unroll
        for (int i = 0; i < 4; i++) { sa = dot2(xq[i], 0x3f803f80u, sa); sb = dot2(xq[i], xq[i], sb); }
      }
      if (k == 4) dma16(SLOT + w * 1024, vc, sc, csrc + cld64);
      if (k == 2) dma16(SLOT + 8192 + w * 1024, vc, sc, csrc + 2 * cld64);
      if (k == 0 && ATT) {
        if (idx >= 110) dma16(SLOT + 16384 + w * 1024, vj, sj, w2_row(73, w) * 1280 + (idx - 107) * 256);
        else dma16(SLOT + 16384 + w * 1024, vc, sc, part == 0 ? csrc + 3 * cld64 : OOB);
      }
      if (k == 3) { sa = dpp_add<0xB1>(sa); sb = dpp_add<0xB1>(sb); }
      if (k == 1) { sa = dpp_add<0x4E>(sa); sb = dpp_add<0x4E>(sb); }
    } else if constexpr (s == 7) {
      if (k == 6) { sa = dpp_add<0x124>(sa); sb = dpp_add<0x124>(sb); }
      if (k == 5) { sa = dpp_add<0x128>(sa); sb = dpp_add<0x128>(sb); }
      if (k == 4) sa = sum_swap16(sa, sb);
      if (k == 3) sa = sum_swap32(sa, sa);
      if (k == 2) {
        sb = __uint_as_float(__builtin_amdgcn_readlane(__float_as_uint(sa), 16)) * (1.f / D);
        sa = __uint_as_float(__builtin_amdgcn_readlane(__float_as_uint(sa), 0)) * (1.f / D);
      }
      if (k == 1) sb = __builtin_amdgcn_rsqf(fmax_(sb - sa * sa, 0.f) + 1e-5f);
      if (k == 0) st32c(__float_as_uint(vl16 ? sb : -sa), sws, vl16 < 32 ? vl16 * 512 : OOB, O_ST + (blockIdx.x * 8 + w) * 4);
    } else if constexpr (s == 8) {
      if (m == 0) r[0] = lds_tr2(ac, 512);
      if (m == 1) r[1] = lds_tr2(ac + 8192, 512);
      if (m == 3 && ATT) r[2] = idx >= 110 ? lds_tr2(aj + 16384, 1024) : lds_tr2(ac + 16384, 512);
      if (m == 6) chunk_mma(false, r[0]);
      if (m == 9) chunk_mma(false, r[1]);
      if (m == 12 && ATT) chunk_mma(false, r[2]);
      if (k == 1) *(LAS float*)(add_s(vl16 >> 2, L_PART + w * 256)) = cacc[0];
      if (k == 0 && ATT) { if (idx >= 110) st128c(r[2], sws, vl16, job_dst(idx + 258, w)); }
    } else if constexpr (s == 9) {
      if (m == 0) cacc[1] = *(LAS float*)(add_s(vl16 >> 2, L_PART + (w | 4) * 256));
      if (m == LW - 1) { if (w < 4 && !(ATT && idx >= 108)) st32c(__float_as_uint(cacc[1] + cacc[0]), sws, vl16 < 256 ? vl16 >> 2 : OOB, O_CS + (part * 5376 + slab * 64 + w * 16) * 4); }
    } else if constexpr (s == 10) {
      if (k == 3) { if (w == 7) { LANDED(ep); ep = rfl(ep) * 0x9E3779B1u + 0x7F4A7C15u; *(LAS unsigned*)(L_RDY + (vl16 >> 2)) = vl16 == 12 * 16 ? ep : 0u; } }
    } else if constexpr (s == 11) {
      if (m == OPT) { if (w == 7) st32c(ep, sws, vl16 == 0 ? 0 : OOB, O_SF + blockIdx.x * 4); }
      if (k == 1) {
        const unsigned ww = opq_s(w);
        bsrc = O_W2F + (opq_s(cg2) * KS2 + (OFIRST ? 5 * (ww & 3) : p2_f0(ww & 3)) + (ww >> 2)) * 5120;
        asm volatile("" : "+s"(bsrc));
      }
    } else if constexpr (s == SP) {
      if (m == OPT) { if (w == 7) ald128c(r[0], sws, vl16, O_SF); }
    } else if constexpr (s == SP + 2) {
      if (m == OPT) {
        if (w == 7) {
          LANDED(r[0]);
          auto all_set = [&](u32x4 f) { return __builtin_amdgcn_ballot_w64(((f[0] ^ ep) | (f[1] ^ ep) | (f[2] ^ ep) | (f[3] ^ ep)) != 0) == 0; };
          if (!all_set(r[0])) {
#pragma unroll 1
            for (unsigned n = 0; n < SPIN_CAP; n++) {
              u32x4 f;
              asm volatile("buffer_load_dwordx4 %0, %1, %2, %3 offen sc1\n\ts_waitcnt vmcnt(0)" : "=&v"(f) : "v"(vl16), "s"(sws), "s"(O_SF) : "memory");
              if (all_set(f)) break;
              __builtin_amdgcn_s_sleep(2);
            }
          }
          if (blockIdx.x == 0) st32c(ep, sws, vl16 == 0 ? 0 : OOB, O_EP);
        }
      }
    } else if constexpr (s == SP + 3) {
      if (m == (ATT ? 1 : LW + 1)) {
        const unsigned ww = opq_s(w);
        if (!ATT) {
          const unsigned j = ww >> 1, mm = ww & 1;
          dma16c(ww < 6 ? L_FE + ww * 1024 : L_ST + mm * 1024, vl16, sws, ww < 6 ? O_CS + (j * 5376 + 1920 + mm * 1728 + cg * 192) * 4 : O_ST + mm * 8192 + r0 * 4);
        } else {
          const unsigned sect = (vl16 * 13) >> 12;
          dma16c(ww < 6 ? L_FE + (ww < 3 ? ww : 3) * 1024 : L_ST + (ww & 1) * 1024, ww < 3 ? vl16 + sect * 2240 : vl16, sws, ww < 3 ? O_CS + ww * 5376 * 4 + cg * 320 : (ww < 6 ? OOB : O_ST + (ww & 1) * 8192 + r0 * 4));
        }
      }
    } else if constexpr (s == SP + 5) {
      if (m == 0) { if (w < (ATT ? 1 : 2)) { const unsigned a = add_s(vl16, L_FE + w * 1024); constexpr unsigned d = ATT ? 1024 : 2048; r[0] = lds_r128(a); r[1] = lds_r128(a + d); r[2] = lds_r128(a + 2 * d); } }
      if (m == 6) {
        if (w < (ATT ? 1 : 2)) {
          const f32x4 t = __builtin_bit_cast(f32x4, r[0]) + __builtin_bit_cast(f32x4, r[1]) + __builtin_bit_cast(f32x4, r[2]);
          if (vl16 < (ATT ? 960 : 768)) *(LAS f32x4*)(L_CS + w * 768 + vl16) = t;
        }
      }
    }
  }
  static constexpr int nwait(int s) {
    constexpr int f[20] = {HALF0 ? 7 : 3, 5, 7, 5, 7, 7, 7, 5, 4, 6, 7, 3, 3, 5, 7, 3, 3, 4, 5, -1};
    constexpr int a[20] = {HALF0 ? 7 : 2, 5, 7, 5, 7, 7, 7, 5, 3, 6, 7, 2, 2, 5, 7, 2, 2, 2, 6, 10};
    return ATT ? a[s] : f[s];
  }
};

template <int SL> DI void wait_ge(unsigned a, unsigned n) {
#pragma unroll 1
  for (unsigned k = 0; k < (SPIN_CAP << 6); k++) {
    const unsigned v = *(volatile LAS unsigned*)a;
    if ((unsigned)rfl(v) >= n) break;
    __builtin_amdgcn_s_sleep(SL);
  }
}
constexpr unsigned L_CNT = L_RDY, L_PRG = L_RDY + 16, L_FIL = L_RDY + 32, L_TAG = L_RDY + 48;

struct P2 {
  rsrc_t sws; int w, panel, cg2; unsigned vo, a_o, a_h, b_o, b_h, rot[4];
  u32x4 A[19][4], B[19][5], E[10][5]; u32x4 xq[2]; u32x2 xd; unsigned xo;
  DI void init(rsrc_t s_, int w_, unsigned vl16, int panel_, int cg2_) {
    sws = s_; w = opq_s(w_); panel = opq_s(panel_); cg2 = opq_s(cg2_);
    const int i = w & 3, f0 = p2_f0(i);
    vo = vl16;
    a_o = O_HF + (panel * KS2 + 5 * i) * 4096; a_h = O_HF + (panel * KS2 + f0) * 4096;
    b_o = O_W2F + (cg2 * KS2 + 5 * i) * 5120; b_h = O_W2F + (cg2 * KS2 + f0) * 5120;
#pragma unroll
    for (int a = 0; a < 4; a++) rot[a] = ((a + w) & 3) * 1024;

  }
  DI bool dummy(int j) { return !seq_is_o(j) && seq_off(j) == 13 && (w & 3) >= 2; }
  DI unsigned soA(int j) { return dummy(j) ? OOB : (seq_is_o(j) ? a_o : a_h) + seq_off(j) * 4096; }
  DI unsigned soB(int j) { return dummy(j) ? OOB : (seq_is_o(j) ? b_o : b_h) + seq_off(j) * 5120; }
  DI void ldA(int j) {
    const unsigned so = soA(j);
#pragma unroll
    for (int q = 0; q < 4; q++) A[j][q] = ld128c(sws, vo, so + rot[q]);
  }
  DI void ldE(int k, int q0 = 0, int q1 = 5) {
    const unsigned so = k < 3 ? (w < 4 ? soB(12 + k) : soB(6 + k)) : soB(k < 6 ? 6 + k : 9 + k);
#pragma unroll
    for (int q = q0; q < q1; q++) E[k][q] = ld128c(sws, vo + (q & 3) * 1024, so + (q >> 2) * 4096);
  }
  template <int k> DI void ldX(rsrc_t sx) {
    if (k == 0) { const unsigned l = vo >> 4; xo = (l & 15) * 1280 + (l & 16) * 2 + (l >> 5) * 16; }
    if (k == 2) { const unsigned l = vo >> 4; xo = (l & 15) * 1280 + (l >> 4) * 8; }
    const unsigned so = (panel * 64 + w * 16) * 1280 + cg2 * 160 + k * 64;
    if constexpr (k < 2) ald128(xq[k], sx, xo, so);
    else asm volatile("buffer_load_dwordx2 %0, %1, %2, %3 offen" : "=v"(xd) : "v"(xo), "s"(sx), "s"(so) : "memory");
  }
  DI void poll(unsigned fo, unsigned long long mask, unsigned tag) {
#pragma unroll 1
    for (unsigned n = 0; n < SPIN_CAP; n++) {
      const unsigned v = ld32c(sws, fo, O_DF);
      if ((__builtin_amdgcn_ballot_w64(fo == OOB || v == tag) & mask) == mask) break;
      __builtin_amdgcn_s_sleep(1);
    }
    asm volatile("" ::: "memory");
  }
  template <bool ATT> DI void run(unsigned tag STAMP_ARG) {
    constexpr int NPRE = 3 + NA0, NHI = NPRE + NH0;
    int l = lane_id(); const int b = blockIdx.x;
    if (w >= 4) {
#pragma unroll
      for (int k = 3; k < NPRE; k++) ldE(k);
      wait_vm<5 * NA0>();
    } else WAIT_VM(0);
    {
      unsigned n;
      asm volatile("s_mov_b64 exec, 1\n\tds_add_rtn_u32 %0, %1, %2\n\ts_mov_b64 exec, -1\n\ts_waitcnt lgkmcnt(0)" : "=&v"(n) : "v"(L_CNT), "v"(1u) : "memory");
      st32c(tag, sws, l == 0 ? 0 : OOB, rfl(n) == 7 ? O_DF + b * 128 : OOB);
    }
    STAMP(4);
    if (w >= 4) {
      const int i = w & 3;
      auto fill = [&](int k) {
        wait_ge<HSL>(L_PRG + i * 4, k + 1);
        const unsigned sa = add_s(vo, bslot_k<ATT>(k % 6, i));
#pragma unroll
        for (int c = 0; c < 5; c++) *(LAS u32x4*)(sa + c * 1024) = E[k][c];
        asm volatile("" ::: "memory");
        if (k == 2 || k == 5 || k == 9) *(volatile LAS unsigned*)(L_FIL + i * 4) = k / 3 + (k < 9);
      };
      wait_ge<1>(L_CNT, 8);
      SB();
#pragma unroll
      for (int k = NPRE; k < NHI && k < 10; k++) ldE(k);
      static_for<0, 10>([&](auto kc) __attribute__((always_inline)) {
        constexpr int k = decltype(kc)::v;
        fill(k);
        if constexpr ((k & 1) && NHI + k / 2 < 10) { SB(); ldE(NHI + k / 2); SB(); }
      });
      WAIT_LGKM0();
      BAR();
      STAMP_DUMP();
      asm volatile("s_endpgm" ::: "memory");
    }
    unsigned fo;
    {
      const unsigned lu = l, hg = lu & 7, r0 = panel * 64, r1 = r0 + 63;
      const unsigned t0 = r0 < 160 ? 0 : ((r0 - 14) * 449) >> 16, t1 = r1 < 160 ? 0 : ((r1 - 14) * 449) >> 16;
      const unsigned c0 = t0 >= 7 ? (t0 - 7) * 8 + 1 : t0 * 8, c1 = t1 >= 7 ? (t1 - 7) * 8 + 1 : t1 * 8;
      const unsigned pa = 144 + (hg & 1) * 56 + (hg & 6) + ((lu & 8) ? c1 : c0);
      const unsigned h0 = w == 3 ? 0 : (5 * w) >> 1, hn = w == 3 ? 8 : 3;
      const unsigned rt = panel >> 1, j = (2 * w + (lu & 3)) * 8 + (rt & 7), xnn = (j * 57) >> 10, pf = (j - 18 * xnn) * 8 + xnn * 2 + (rt >> 3);
      const unsigned fa = (hg - h0 < hn) ? pa * 128 : OOB, ff = (lu < 19) ? pf * 128 : OOB;
      fo = lu < 16 ? fa : ff;
    }
    f32x4 acc[4][5];
    LANDED(xq[0]); LANDED(xq[1]); LANDED(xd);
#pragma unroll
    for (int k = 0; k < 2; k++) {
      unsigned a0 = xq[k][0], a1 = xq[k][1], b0 = xq[k][2], b1 = xq[k][3];
      swap16(a0, b0); swap16(a1, b1);
      acc[0][2 * k] = f32x4{bf_lo(a0), bf_hi(a0), bf_lo(a1), bf_hi(a1)}; acc[0][2 * k + 1] = f32x4{bf_lo(b0), bf_hi(b0), bf_lo(b1), bf_hi(b1)};
    }
    acc[0][4] = f32x4{bf_lo(xd[0]), bf_hi(xd[0]), bf_lo(xd[1]), bf_hi(xd[1])};
    unsigned fil = 0, pv = 0;
    const unsigned pa = opq_v(L_PRG + w * 4);
    unsigned sn = add_s(vo, bslot_k<ATT>(0, w));
    auto step = [&](auto jc, auto lac) __attribute__((always_inline)) {
      constexpr int j = decltype(jc)::v, la = decltype(lac)::v;
      constexpr bool cur = j < 12 || j >= 15, nxt = j + 1 < 12 || (j + 1 >= 15 && j < 18);
      const unsigned sc = sn;
      if (nxt) sn = add_s(vo, bslot_k<ATT>(j + 1 < 12 ? (j + 1) % 6 : (j + 1 - 15) % 6, w));
      if (j == 5 || j == 8 || j == 14) { constexpr unsigned need = j == 5 ? 1 : (j == 8 ? 2 : 3); if ((unsigned)rfl(fil) < need) wait_ge<1>(L_FIL + w * 4, need); }
      P2_STAMP(j);
      asm volatile("" :: "v"(A[j][0]), "v"(A[j][1]), "v"(A[j][2]), "v"(A[j][3]));
      P2_STAMP(32 + j);
      SB();
      static_for<0, 20>([&](auto mc) __attribute__((always_inline)) {
        constexpr int m = decltype(mc)::v, c = m >> 2, a = m & 3;
        if constexpr (cur) mma<j == 0 && a != 0>(acc[a][c], B[j][c], A[j][a]); else mma<false>(acc[a][c], E[j - 12][c], A[j][a]);
        SB();
        if constexpr (a == 0) {
          if constexpr (cur && c + 2 < 5) B[j][c + 2] = lds_r128(sc + (c + 2) * 1024);
          if constexpr (nxt && c + 2 >= 5) B[j + 1][c - 3] = lds_r128(sn + (c - 3) * 1024);
        }
        if constexpr (!OFIRST && j == 0 && a == 1 && c < 4) A[2][c] = ld128c(sws, vo, soA(2) + rot[c]);
        if constexpr (la >= 0 && a == 2 && c < 4) A[la][c] = ld128c(sws, vo, soA(la) + rot[c]);
        if constexpr (m == 17 && j <= 9) *(volatile LAS unsigned*)pa = j + 1;
        if constexpr (m == 15 && (j == 4 || j == 7 || j == 13)) fil = *(volatile LAS unsigned*)(pa + 16);
        if constexpr (!OFIRST && j == 7 && m == 19) pv = ld32c(sws, fo, O_DF);
        SB();
      });
    };
#define STEP(j, la) step(IC<j>{}, IC<la>{})
    if (PRIO) __builtin_amdgcn_s_setprio(PRIO);
    wait_ge<1>(L_CNT, 8);
    B[0][0] = lds_r128(sn); B[0][1] = lds_r128(sn + 1024);
    asm volatile("s_nop 1" : "+v"(acc[0][0]), "+v"(acc[0][1]), "+v"(acc[0][2]), "+v"(acc[0][3]), "+v"(acc[0][4]));
    poll(fo, OFIRST ? 0xffffull : 0x70000ull, tag);
    STAMP(5);
    ldA(0); ldA(1);
    if (OFIRST) { ldA(2); ldA(3); }
    if (OFIRST) {
      STEP(0, -1); STEP(1, 4); STEP(2, -1); STEP(3, -1); STEP(4, -1);
      poll(fo, 0x70000ull, tag);
      STAMP(6);
      ldA(5); ldA(6); ldA(7); ldA(8);
      STEP(5, -1); STEP(6, 9); STEP(7, 10); STEP(8, 11); STEP(9, 12); STEP(10, 13);
    } else {
      STEP(0, 3); STEP(1, 4); STEP(2, 5); STEP(3, 6); STEP(4, 7); STEP(5, 8); STEP(6, 9); STEP(7, 10); STEP(8, 11); STEP(9, 12); STEP(10, 13);
      if ((__builtin_amdgcn_ballot_w64(fo == OOB || pv == tag) & 0xffffull) != 0xffffull) poll(fo, 0xffffull, tag);
      asm volatile("" ::: "memory");
      STAMP(6);
    }
    STEP(11, 14); STEP(12, 15); STEP(13, 16); STEP(14, 17); STEP(15, 18); STEP(16, -1); STEP(17, -1); STEP(18, -1);
    MMA_DRAIN();
    P2_STAMP(19);
    STAMP(7);
#pragma unroll
    for (int a = 1; a < 4; a++) {
      const unsigned sd = add_s(vo, bslot_k<ATT>(2 + a, w));
#pragma unroll
      for (int c = 0; c < 5; c++) *(LAS f32x4*)(sd + c * 1024) = acc[a][c];
    }
    unsigned ra[3];
#pragma unroll
    for (int k = 1; k < 4; k++) ra[k - 1] = add_s(vo, bslot_k<ATT>(2 + k, (w - k) & 3));
    l = lane_id();
    const int lg = l >> 4;
    u16* op = kargs()->out + (panel * 64 + w * 16 + (l & 15)) * D + cg2 * 80;
    WAIT_LGKM0();
    BAR();
    u32x2 ov[5];
#pragma unroll
    for (int c = 0; c < 5; c++)
      ov[c] = pk4((acc[0][c] + *(LAS f32x4*)(ra[0] + c * 1024)) + (*(LAS f32x4*)(ra[1] + c * 1024) + *(LAS f32x4*)(ra[2] + c * 1024)));
#pragma unroll
    for (int c = 0; c < 4; c += 2) {
      unsigned a0 = ov[c][0], a1 = ov[c][1], b0 = ov[c + 1][0], b1 = ov[c + 1][1];
      swap16(a0, b0); swap16(a1, b1);
      *(u32x4*)(op + (c + (lg & 1)) * 16 + (lg >> 1) * 8) = u32x4{a0, a1, b0, b1};
    }
    *(u32x2*)(op + 64 + 4 * lg) = ov[4];
    STAMP(8);
    STAMP_DUMP();
  }
};

#define P2MAP 1
#define FPRE 4
#define APRE 1
#define FG 0
constexpr int ffn_op(int m) {
  constexpr int t[3][24] = {{0, 1, 20, 2, 3, 4, 5, 21, 6, 7, 8, 9, 22, 10, 11, 12, 13, 14, 15, 30, -1, 31, -1, -1},
                            {0, 1, 2, 3, 4, 5, 6, 7, 8, 9, 10, 11, 12, 13, 14, 15, 20, 21, 22, 30, -1, 31, -1, -1},
                            {20, 0, 21, 1, 22, 2, 3, 4, 5, 6, 7, 8, 9, 10, 11, 12, 13, 14, 15, 30, -1, 31, -1, -1}};
  return t[FG][m];
}
DI void ffn_wg(const P& p, rsrc_t sws, int rt, int cg, int panel, int cg2, int w, int l STAMP_ARG) {
  int lr = l & 15, lg = l >> 4; const int wm = w >> 2, wn = w & 3;
  constexpr unsigned XB = 32768, WB = 65536, WS = 24576;
  rsrc_t sx = mk_rsrc(p.x, S * D * 2);
  rsrc_t sw = mk_rsrc(wm ? p.w3 : p.w1, D * F * 2);
  unsigned wv[3];
  const unsigned xs0 = (rt * 128 + 16 * w) * 1280;
  const unsigned xdst = w * 4096, wdst = WB + wm * 12288 + wn * 3072;
  unsigned xv = lg * 1280 + ((lr ^ lg) << 4);
  auto dma_x = [&](int slab, int i) {
    const unsigned vo = i == 0 ? xv : (i == 1 ? xor_c<64>(xv) : (i == 2 ? xor_c<128>(xv) : xor_c<192>(xv)));
    if (i & 1) dma_more<1024>(vo, sx, xs0 + i * 5120 - 1024 + slab * 256);
    else dma16(xdst + (slab & 1) * XB + i * 1024, vo, sx, xs0 + i * 5120 + slab * 256);
  };
  auto dma_w = [&](int step, int i) {
    if (i == 0) dma16(wdst + (step % 3) * WS, wv[0], sw, step * (32 * 3456));
    else if (i == 1) dma_more<1024>(wv[1], sw, step * (32 * 3456));
    else dma_more<2048>(wv[2], sw, step * (32 * 3456));
  };
  auto dma_x0 = [&](int half) {
#pragma unroll
    for (int i = 0; i < 4; i++) {
      if (!HALF0) dma_x(0, i);
      else dma16m(xdst + i * 1024, xv ^ (i << 6), sx, xs0 + i * 5120, ((i >> 1) ^ half) ? 0xff00ff00ff00ff00ull : 0x00ff00ff00ff00ffull);
    }
  };
  dma_x0(0);
  SB();
#pragma unroll
  for (int i = 0; i < 3; i++) {
    unsigned u = 64 * i + l, rl = (u * 171) >> 12, pos = u - 24 * rl, row = 8 * wn + rl;
    unsigned sk = ((rl >> 1) & 1) | ((wn & 1) << 1);
    wv[i] = row * 3456 + cg * 384 + ((pos ^ (sk << 1)) << 4) - i * 1024;
  }
#pragma unroll
  for (int i = 0; i < (PRO == 2 ? 3 : 6); i++) dma_w(i / 3, i % 3);
  if (PRO == 0) { if (HALF0) dma_x0(1); dma_w(2, 0); dma_w(2, 1); dma_w(2, 2); }
  SB();
  STAMP(9);
  Svc<false, 24> sv;
  sv.sws = sws; sv.sx = sx; sv.w = w; sv.vl16 = l * 16;
  sv.setup(p);
  unsigned xa = lr * 256 + ((lg ^ lr) << 4) + wm * 16384, wa[3];
  {
    int q = lr >> 2, pp = l & 3, row = 8 * lg + q, sk = ((q >> 1) & 1) | ((lg & 1) << 1);
#pragma unroll
    for (int j = 0; j < 3; j++) wa[j] = WB + row * 384 + (((3 * wn + j) ^ sk) << 5) + pp * 8;
  }
  P2 p2;
  STEP_DECL
  f32x4 acc[4][6];
  u32x4 xf[2][4]; u32x2 wf[2][12];
  SB();
  STAMP(10);
  asm volatile("" : "+v"(xa), "+v"(wa[0]), "+v"(wa[1]), "+v"(wa[2]));
  if (PRO == 2) { dma_w(1, 0); dma_w(1, 1); dma_w(1, 2); if (HALF0) dma_x0(1); dma_w(2, 0); dma_w(2, 1); dma_w(2, 2); }
  wait_vm<3 + (PRO != 1 ? sv.nwait(0) : 0)>();
  BAR();
  STAMP(1);
#pragma unroll
  for (int i = 0; i < 4; i++) xf[0][i] = lds_r128(xa + i * 4096);
#pragma unroll
  for (int j = 0; j < 12; j++) wf[0][j] = lds_tr(wa[(j >> 1) % 3] + (j / 6) * 12288 + (j & 1) * 1536);
  if (PRO == 1) { if (HALF0) dma_x0(1); dma_w(2, 0); dma_w(2, 1); dma_w(2, 2); }
  static_for<0, 20>([&](auto sc) __attribute__((always_inline)) {
    constexpr int s = decltype(sc)::v, t = s & 3, sl = s >> 2, c = s & 1, n = c ^ 1;
    STEP_STAMP(42 + s);
    if constexpr (sv.nwait(s) >= 0) wait_vm<sv.nwait(s) >= 0 ? sv.nwait(s) : 0>();
    STEP_STAMP(21 + s);
    if (s < 19) { WAIT_LGKM0(); BAR(); }
    STEP_STAMP(s);
    SB();
    const unsigned xb = xor_c<(((t + 1) & 3) << 6)>(xa), xo = (((s + 1) >> 2) & 1) * XB, wo = ((s + 1) % 3) * WS;
    static_for<0, 24>([&](auto mc) __attribute__((always_inline)) {
      constexpr int m = decltype(mc)::v, j = m >> 2, i = m & 3, op = ffn_op(m);
      mma<s == 0>(acc[i][j], u32x4{wf[c][2 * j][0], wf[c][2 * j][1], wf[c][2 * j + 1][0], wf[c][2 * j + 1][1]}, xf[c][i]);
      SB();
      if constexpr (op >= 0 && op < 4 && s < 19) xf[n][op] = lds_r128(xb + xo + op * 4096);
      if constexpr (op >= 4 && op < 16 && s < 19) { constexpr int k = op - 4; wf[n][k] = lds_tr(wa[(k >> 1) % 3] + wo + (k / 6) * 12288 + (k & 1) * 1536); }
      if constexpr (op >= 20 && op < 23 && s + 3 < 20) dma_w(s + 3, op - 20);
      if constexpr (op >= 30 && t < 2 && sl < 4) dma_x(sl + 1, 2 * t + op - 30);
      sv.template gap<s, m>(rt * 128, cg, cg2);
      if constexpr (s == 9 && m == 22) p2.init(sws, w, sv.vl16, panel, cg2);
      if constexpr ((s == 10 || s == 11 || s == 14) && m == sv.OPT + 1) { if (w < 4) p2.template ldX<s == 14 ? 2 : s - 10>(sx); }
      if constexpr (s == 19 && m % 4 == 1 && m < 20) p2.ldE(0, m / 4, m / 4 + 1);
      SB();
    });
  });
  MMA_DRAIN();
  STEP_STAMP(20);
  STEP_DUMP();
  STAMP(2);
  WAIT_LGKM0();
  BAR();
  l = lane_id(); lr = l & 15; lg = l >> 4;
  f32x4 cs[6];
#pragma unroll
  for (int j = 0; j < 6; j++) cs[j] = *(LAS f32x4*)(L_CS + (j / 3) * 768 + ((3 * wn + j % 3) * 16 + 4 * lg) * 4);
  f32x2 stt[4];
  {
    const unsigned t = add_s(lr * 4, L_ST + wm * 256);
#pragma unroll
    for (int i = 0; i < 4; i++) stt[i] = f32x2{*(LAS float*)(t + i * 64), *(LAS float*)(t + 1024 + i * 64)};
  }
  const unsigned tag = rfl(*(LAS unsigned*)L_TAG);
  static_for<0, 5>([&](auto jc) __attribute__((always_inline)) { bpiece<false, 2, decltype(jc)::v>(sws, (OFIRST && w >= 4) ? p2.b_h : sv.bsrc + 4 * 5120, w, l * 16); });
  const int ksb = 20 + 6 * cg + 3 * (wn >> 1);
  const unsigned hb = O_HF + ((rt * 2 + wm) * KS2 + ksb) * 4096;
  u32x2 h2[2];
  static_for<0, 4>([&](auto ic) __attribute__((always_inline)) {
    constexpr int i = decltype(ic)::v;
    const float nmu = stt[i][0], c1 = -stt[i][1] * 1.44269504089f, ic2 = __builtin_amdgcn_rcpf(stt[i][1] * stt[i][1]);
    const f32x2 nmu2 = {nmu, nmu}, c12 = {c1, c1}, ic22 = {ic2, ic2};
    f32x2 g[6], e[6];
#pragma unroll
    for (int q = 0; q < 6; q++) {
      const int j = q >> 1, r = q & 1;
      g[q] = __builtin_elementwise_fma(f32x2{cs[j][2 * r], cs[j][2 * r + 1]}, nmu2, f32x2{acc[i][j][2 * r], acc[i][j][2 * r + 1]});
      e[q] = g[q] * c12;
    }
    SB();
    if (i == 0) p2.ldE(1, 0, 2); else if (i == 1) p2.ldE(1, 4, 5); else if (i == 2) p2.ldE(2, 2, 3);
    SB();
#pragma unroll
    for (int q = 0; q < 6; q++) {
      const int j = q >> 1, r = q & 1;
      e[q] = f32x2{__builtin_amdgcn_exp2f(e[q][0]), __builtin_amdgcn_exp2f(e[q][1])};
      g[q] *= __builtin_elementwise_fma(f32x2{cs[3 + j][2 * r], cs[3 + j][2 * r + 1]}, nmu2, f32x2{acc[i][3 + j][2 * r], acc[i][3 + j][2 * r + 1]});
    }
    SB();
#pragma unroll
    for (int q = 0; q < 6; q++) e[q] = __builtin_elementwise_fma(e[q], ic22, ic22);
    SB();
    if (i == 0) p2.ldE(1, 2, 4); else if (i == 1) p2.ldE(2, 0, 2); else if (i == 2) p2.ldE(2, 3, 5);
    SB();
#pragma unroll
    for (int q = 0; q < 6; q++) e[q] = f32x2{__builtin_amdgcn_rcpf(e[q][0]), __builtin_amdgcn_rcpf(e[q][1])};
    SB();
#pragma unroll
    for (int q = 0; q < 6; q++) g[q] *= e[q];
    unsigned hv[6];
#pragma unroll
    for (int q = 0; q < 6; q++) hv[q] = pk(g[q][0], g[q][1]);
    st128c(u32x4{hv[0], hv[1], hv[2], hv[3]}, sws, l * 16, hb + (wn & 1) * 4096 + i * 1024);
    h2[i & 1] = u32x2{hv[4], hv[5]};
    if (i & 1) {
      unsigned a0 = h2[0][0], a1 = h2[0][1], b0 = h2[1][0], b1 = h2[1][1];
      swap16(a0, b0); swap16(a1, b1);
      st128c(u32x4{a0, a1, b0, b1}, sws, (lg & 1) * 1024 + ((lg >> 1) * 16 + lr) * 16, hb + 8192 + (i - 1) * 1024 + (wn & 1) * 512);
    }
    SB();
  });
  SB();
  STAMP(3);
  p2.run<false>(tag STAMP_PASS);
}

constexpr int att_op(int m) {
  constexpr int t[20] = {0, 1, 20, 2, 3, 4, 5, 6, 21, 7, 8, 9, 10, 11, 12, 30, -1, 31, -1, 32};
  return t[m];
}
DI void att_wg(const P& p, rsrc_t sws, int rt, int hg, int panel, int cg2, int w, int l STAMP_ARG) {
  int lr = l & 15, lg = l >> 4; const int mh = w >> 2, h = w & 3;
  constexpr unsigned XB = 40960, WB = 81920, WS = 16384;
  rsrc_t sx = mk_rsrc(p.x, S * D * 2);
  rsrc_t sw = mk_rsrc(p.wqkv, D * 3 * D * 2);
  unsigned wv[2];
  const int R0 = 146 * rt;
  const unsigned xs0 = (R0 + 20 * w) * 1280, xdst = w * 5120, wdst = WB + w * 2048;
  unsigned xv = lg * 1280 + ((lr ^ lg) << 4);
  auto dma_x = [&](int slab, int i) {
    const unsigned vo = xor_s(xv, ((w + i) & 3) << 6), so = xs0 + i * 5120 + slab * 256;
    if (i == 0 || i == 3) dma16(xdst + (slab & 1) * XB + i * 1024, vo, sx, so);
    else if (i == 2) dma_more<2048>(vo, sx, so - 2048);
    else dma_more<1024>(vo, sx, so - 1024);
  };
  auto dma_w = [&](int step, int i) {
    if (i == 0) dma16(wdst + (step % 3) * WS, wv[0], sw, step * (32 * 3840));
    else dma_more<1024>(wv[1], sw, step * (32 * 3840));
  };
  auto dma_x0 = [&](int half) {
#pragma unroll
    for (int i = 0; i < 5; i++) {
      if (!HALF0) dma_x(0, i);
      else dma16m(xdst + i * 1024, xv ^ (((w + i) & 3) << 6), sx, xs0 + i * 5120, ((((w + i) >> 1) ^ half) & 1) ? 0xff00ff00ff00ff00ull : 0x00ff00ff00ff00ffull);
    }
  };
  dma_x0(0);
  SB();
#pragma unroll
  for (int i = 0; i < 2; i++) {
    const unsigned rl = 2 * i + (l >> 5), u = (l & 31) ^ ((rl | ((w & 2) << 1)) << 1);
    const unsigned o = u >= 21 ? 2560 - 21 * 16 : (u >= 11 ? 1280 - 11 * 16 : 0), ch = (u == 10 || u == 31) ? u - 1 : u;
    wv[i] = (4 * w + rl) * 3840 + hg * 160 + o + ch * 16 - i * 1024;
  }
#pragma unroll
  for (int i = 0; i < (PRO == 2 ? 2 : 4); i++) dma_w(i >> 1, i & 1);
  if (PRO == 0) { if (HALF0) dma_x0(1); dma_w(2, 0); dma_w(2, 1); }
  SB();
  STAMP(9);
  Svc<true, 20> sv;
  sv.sws = sws; sv.sx = sx; sv.w = w; sv.vl16 = l * 16;
  sv.setup(p);
  unsigned xa = lr * 256 + ((lg ^ lr) << 4) + mh * 5 * 4096, wa[4];
  {
    const unsigned q = lr >> 2, pp = l & 3, fk = (q | ((lg & 1) << 2)) << 5, base = WB + (8 * lg + q) * 512;
    const unsigned o0 = 40 * h + 8 * pp, o[4] = {o0, o0 + 176, 40u * h + 32 + (pp == 0 ? 0 : (pp == 1 ? 176 : 336)), o0 + 336};
#pragma unroll
    for (int j = 0; j < 4; j++) wa[j] = base + (o[j] ^ fk);
  }
  P2 p2;
  STEP_DECL
  f32x4 acc[5][4], mk0, mk1;
  unsigned sel0, sel1, fo[4], vlw, vlr;
  constexpr unsigned L_VL = 16384, L_VZ = L_VL + 4 * 1408;
  u32x4 xf[2][5]; u32x2 wf[2][8];
  SB();
  STAMP(10);
  asm volatile("" : "+v"(xa), "+v"(wa[0]), "+v"(wa[1]), "+v"(wa[2]), "+v"(wa[3]));
  if (PRO == 2) { dma_w(1, 0); dma_w(1, 1); if (HALF0) dma_x0(1); dma_w(2, 0); dma_w(2, 1); }
  wait_vm<2 + (PRO != 1 ? sv.nwait(0) : 0)>();
  BAR();
  STAMP(1);
#pragma unroll
  for (int i = 0; i < 5; i++) xf[0][i] = lds_r128(xa + i * 4096);
#pragma unroll
  for (int j = 0; j < 8; j++) wf[0][j] = lds_tr(wa[j >> 1] + (j & 1) * 2048);
  if (PRO == 1) { if (HALF0) dma_x0(1); dma_w(2, 0); dma_w(2, 1); }
  static_for<0, 20>([&](auto sc) __attribute__((always_inline)) {
    constexpr int s = decltype(sc)::v, t = s & 3, sl = s >> 2, c = s & 1, n = c ^ 1;
    STEP_STAMP(42 + s);
    if constexpr (sv.nwait(s) >= 0) wait_vm<sv.nwait(s) >= 0 ? sv.nwait(s) : 0>();
    STEP_STAMP(21 + s);
    WAIT_LGKM0(); BAR();
    STEP_STAMP(s);
    SB();
    const unsigned xb = xor_c<(((t + 1) & 3) << 6)>(xa), xo = (((s + 1) >> 2) & 1) * XB, wo = ((s + 1) % 3) * WS;
    static_for<0, 20>([&](auto mc) __attribute__((always_inline)) {
      constexpr int m = decltype(mc)::v, j = m / 5, i = m % 5, op = att_op(m);
      const u32x4 wfr = {wf[c][2 * j][0], wf[c][2 * j][1], wf[c][2 * j + 1][0], wf[c][2 * j + 1][1]};
      if (j < 3) mma<s == 0>(acc[i][j], wfr, xf[c][i]); else mma<s == 0>(acc[i][j], xf[c][i], wfr);
      SB();
      if constexpr (op >= 0 && op < 5 && s < 19) xf[n][op] = lds_r128(xb + xo + op * 4096);
      if constexpr (op >= 5 && op < 13 && s < 19) { constexpr int k = op - 5; wf[n][k] = lds_tr(wa[k >> 1] + wo + (k & 1) * 2048); }
      if constexpr (op >= 20 && op < 22 && s + 3 < 20) dma_w(s + 3, op - 20);
      if constexpr (op >= 30 && sl < 4 && (t == 0 || (t == 1 && op < 32))) dma_x(sl + 1, 3 * t + op - 30);
      sv.template gap<s, m>(R0, hg, cg2);
      if constexpr (s == 9 && m == 18) p2.init(sws, w, sv.vl16, panel, cg2);
      if constexpr ((s == 10 || s == 11 || s == 14) && m == sv.OPT + 1) { if (w < 4) p2.template ldX<s == 14 ? 2 : s - 10>(sx); }
      if constexpr (s == 19 && m % 4 == 1) p2.ldE(0, m / 4, m / 4 + 1);
      if constexpr (s == 12 && m == 16) {
        const int ll = lane_id(), de = (ll & 14) - 4 * (ll >> 4);
        const float NEG = -__builtin_inff();
        const bool odd = ll & 1;
        const float p0 = de <= -2 ? 0.f : NEG, p1 = de <= 0 ? 0.f : NEG, c0 = de >= 0 ? 0.f : NEG, c1 = de >= 2 ? 0.f : NEG;
        mk0 = f32x4{odd ? NEG : p0, odd ? p0 : NEG, odd ? NEG : p1, odd ? p1 : NEG};
        mk1 = f32x4{odd ? NEG : c0, odd ? c0 : NEG, odd ? NEG : c1, odd ? c1 : NEG};
        sel0 = odd ? 0x01000c0cu : 0x0c0c0100u; sel1 = odd ? 0x03020c0cu : 0x0c0c0302u;
        asm volatile("" : "+v"(mk0), "+v"(mk1), "+v"(sel0), "+v"(sel1));
      }
      if constexpr ((s == 13 || s == 15) && (m == 16 || m == 18)) {
        constexpr int q = (s == 15 ? 2 : 0) + (m == 18);
        const unsigned ll = lane_id(), lo = opq_s(rt == 0 ? 0 : R0 + 14);
        const unsigned row = opq_s(R0 + mh * 80) + (ll & 15) + (q == 0 ? 64 : (q == 1 ? 32 + (ll & 16) : (q == 2 ? (ll & 16) : (ll & 48))));
        const unsigned o = ((row >> 6) & 63) * (KS2 * 4096) + ((row & 63) << 6) - (row & 15) * 48 + (q == 3 ? 0 : (ll >> 5) * 256);
        fo[q] = row - lo < S - lo ? o : OOB;
        asm volatile("" : "+v"(fo[q]));
      }
      if constexpr (s == 16 && m == 16) {
        const unsigned ll = lane_id(), b0 = opq_s(L_VL + h * 1408 + mh * 640);
        vlw = (ll >> 4) == 2 ? b0 + 128 + (ll & 15) * 8 : L_VZ + 512 + (ll & 15) * 8;
        vlr = b0 + (ll >> 2) * 8;
        asm volatile("" : "+v"(vlw), "+v"(vlr));
      }
      SB();
    });
  });
  MMA_DRAIN();
  STEP_STAMP(20);
  STEP_DUMP();
  STAMP(2);
  WAIT_LGKM0();
  BAR();
  l = lane_id(); lr = l & 15; lg = l >> 4;
  const unsigned tg = add_s(l & 48, L_ST + mh * 320), tr = add_s(lr * 4, L_ST + mh * 320);
  const unsigned cg_ = add_s(l & 48, L_CS + h * 80);
  const f32x4 csq = *(LAS f32x4*)cg_, csk = *(LAS f32x4*)(cg_ + 320);
  const f32x4 csl = *(LAS f32x4*)(add_s((lg < 3 ? lg : 2) * 320, L_CS + h * 80 + 64));
  const float csv = *(LAS float*)(add_s(lr * 4, L_CS + h * 80 + 640));
  float mu[5], rs[5]; f32x4 mu4[5], rs4[5];
#pragma unroll
  for (int i = 4; i >= 0; i--) {
    mu[i] = *(LAS float*)(tr + i * 64); rs[i] = *(LAS float*)(tr + 1024 + i * 64);
    mu4[i] = *(LAS f32x4*)(tg + i * 64); rs4[i] = *(LAS f32x4*)(tg + 1024 + i * 64);
  }
  const unsigned tag = rfl(*(LAS unsigned*)L_TAG);
  static_for<0, 5>([&](auto jc) __attribute__((always_inline)) { bpiece<true, 2, decltype(jc)::v>(sws, (OFIRST && w >= 4) ? p2.b_h : sv.bsrc + 4 * 5120, w, sv.vl16); });
  auto nrm = [](f32x4 a, f32x4 c, float m) { return f32x4{__builtin_fmaf(c[0], m, a[0]), __builtin_fmaf(c[1], m, a[1]), __builtin_fmaf(c[2], m, a[2]), __builtin_fmaf(c[3], m, a[3])}; };
  u32x4 kq[6], vq[5], qf[5];
  const bool g0 = lg == 0;
  *(LAS u32x2*)(L_VZ + (sv.vl16 >> 1)) = u32x2{0u, 0u};
  static_for<0, 5>([&](auto ic) __attribute__((always_inline)) {
    constexpr int ii = decltype(ic)::v, i = 4 - ii;
    const u32x2 k0 = pk4(nrm(acc[i][1], csk, mu[i]) * rs[i]);
    const u32x2 L = pk4(nrm(acc[i][2], csl, mu[i]) * (g0 ? 1.f : rs[i]));
    *(LAS u32x2*)(vlw + i * 128) = L;
    const u32x2 q0 = pk4(nrm(acc[i][0], csq, mu[i]));
    qf[i] = u32x4{q0[0], q0[1], g0 ? L[0] : 0u, g0 ? L[1] : 0u};
    unsigned x0 = L[0], y0 = k0[0], x1 = L[1], y1 = k0[1];
    swap16(x0, y0); swap16(x1, y1);
    kq[i + 1] = u32x4{k0[0], k0[1], y0, y1};
    const u32x2 v = pk4(f32x4{__builtin_fmaf(mu4[i][0], csv, acc[i][3][0]), __builtin_fmaf(mu4[i][1], csv, acc[i][3][1]), __builtin_fmaf(mu4[i][2], csv, acc[i][3][2]), __builtin_fmaf(mu4[i][3], csv, acc[i][3][3])} * rs4[i]);
    vq[i][2] = v[0]; vq[i][3] = v[1];
    if constexpr (ii == 0) {
      *(LAS u32x4*)(add_s(sv.vl16, L_EX + w * 2048)) = kq[5];
      *(LAS u32x2*)(add_s(sv.vl16 >> 1, L_EX + w * 2048 + 1024)) = v;
    } else { vq[i + 1][0] = v[0]; vq[i + 1][1] = v[1]; }
    SB(); p2.ldE(1, ii, ii + 1); SB();
  });
  const u32x4 ones = {0x3f803f80u, 0x3f803f80u, 0x3f803f80u, 0x3f803f80u};
  const f32x4 z = {0, 0, 0, 0};
  f32x4 s0[5], s1[5];
#pragma unroll
  for (int i = 4; i >= 0; i--) {
    s1[i] = mfma(kq[i + 1], qf[i], mk1);
    if (i > 0) s0[i] = mfma(kq[i], qf[i], mk0);
  }
  WAIT_LGKM0();
  BAR();
  u32x4 vl[5];
  {
    kq[0] = lds_r128(add_s(sv.vl16, L_EX + h * 2048));
    const u32x2 v = *(LAS u32x2*)(add_s(sv.vl16 >> 1, L_EX + h * 2048 + 1024)); vq[0][0] = v[0]; vq[0][1] = v[1];
#pragma unroll
    for (int i = 0; i < 5; i++) {
      const unsigned a = (l & 3) == (i & 3) ? vlr + i * 128 : L_VZ;
      const u32x2 lo = lds_tr(a), hi = lds_tr((l & 3) == (i & 3) ? a + 128 : L_VZ);
      vl[i] = u32x4{lo[0], lo[1], hi[0], hi[1]};
    }
    if (mh == 0) {
      const float NEG = -__builtin_inff();
      mk0 = f32x4{NEG, NEG, NEG, NEG}; vl[0][0] = 0; vl[0][1] = 0;
    }
  }
  s0[0] = mfma(kq[0], qf[0], mk0);
  f32x2 e[5][2]; float mx[5];
#pragma unroll
  for (int i = 4; i >= 0; i--) {
    e[i][0] = f32x2{fmax_(s0[i][0], s0[i][1]), fmax_(s0[i][2], s0[i][3])};
    e[i][1] = f32x2{fmax_(s1[i][0], s1[i][1]), fmax_(s1[i][2], s1[i][3])};
    mx[i] = fmax_(fmax3_(e[i][0][0], e[i][0][1], e[i][1][0]), e[i][1][1]);
  }
#pragma unroll
  for (int i = 4; i > 0; i -= 2) {
    unsigned u0 = __float_as_uint(mx[i]), u1 = __float_as_uint(mx[i - 1]);
    swap16(u0, u1);
    u0 = __float_as_uint(fmax_(__uint_as_float(u0), __uint_as_float(u1))); u1 = u0;
    swap32(u0, u1);
    u0 = __float_as_uint(fmax_(__uint_as_float(u0), __uint_as_float(u1))); u1 = u0;
    swap16(u0, u1);
    mx[i] = __uint_as_float(u0); mx[i - 1] = __uint_as_float(u1);
  }
  mx[0] = red_max_g(mx[0]);
  SB();
  p2.ldE(2);
  SB();
  u32x4 pf[5];
#pragma unroll
  for (int i = 4; i >= 0; i--) {
    const float cx = rs[i] * (0.22360679775f * 1.44269504089f), nm = -mx[i] * cx;
    const f32x2 t0 = __builtin_elementwise_fma(e[i][0], f32x2{cx, cx}, f32x2{nm, nm}), t1 = __builtin_elementwise_fma(e[i][1], f32x2{cx, cx}, f32x2{nm, nm});
    const unsigned c0 = pk(__builtin_amdgcn_exp2f(t0[0]), __builtin_amdgcn_exp2f(t0[1])), c1 = pk(__builtin_amdgcn_exp2f(t1[0]), __builtin_amdgcn_exp2f(t1[1]));
    pf[i] = u32x4{__builtin_amdgcn_perm(0u, c0, sel0), __builtin_amdgcn_perm(0u, c0, sel1), __builtin_amdgcn_perm(0u, c1, sel0), __builtin_amdgcn_perm(0u, c1, sel1)};
  }
  SB();
  f32x4 sm[5], o[5], ola = z, olb;
#pragma unroll
  for (int i = 4; i >= 0; i--) sm[i] = mfma(ones, pf[i], z);
#pragma unroll
  for (int i = 4; i >= 0; i--) {
    o[i] = mfma(vq[i], pf[i], z);
    if (i == 4) olb = mfma(vl[4], pf[i], z); else ola = mfma(vl[i], pf[i], ola);
  }
  SB();
  const unsigned Pp = 2 * hg + (h >> 1);
  float inv[5]; u32x2 O[5];
#pragma unroll
  for (int i = 4; i >= 0; i--) { inv[i] = __builtin_amdgcn_rcpf(sm[i][0]); O[i] = pk4(o[i] * inv[i]); }
  const unsigned so_m = O_HF + Pp * 4096 + (h & 1) * 512, so_l = O_HF + (16 + (Pp >> 2)) * 4096 + (Pp & 3) * 256 + (h & 1) * 8;
  st64c(O[4], sws, fo[0] + ((l >> 1) & 8), so_m);
  st64c(pk4(olb * inv[4]), sws, g0 ? fo[0] : OOB, so_l);
#pragma unroll
  for (int i = 2; i >= 0; i -= 2) {
    unsigned a0 = O[i][0], a1 = O[i][1], b0 = O[i + 1][0], b1 = O[i + 1][1];
    swap16(a0, b0); swap16(a1, b1);
    st128c(u32x4{a0, a1, b0, b1}, sws, fo[2 - i / 2], so_m);
  }
  {
    const float ia = (l & 16) ? inv[1] : inv[0], ib = (l & 16) ? inv[3] : inv[2];
    st64c(pk4(ola * ((l & 32) ? ib : ia)), sws, fo[3], so_l);
  }
  SB();
  STAMP(3);
  p2.template run<true>(tag STAMP_PASS);
}

__global__ __launch_bounds__(512, 2) void fused(P p) {
  const int w = rfl(threadIdx.x >> 6); int l = threadIdx.x & 63;
  const int b = blockIdx.x, x = b & 7, bi = b >> 3, xm = x & 1, xn = x >> 1;
  STAMP(0);
  {
    unsigned long long pc;
    asm volatile("s_getpc_b64 %0" : "=s"(pc));
    asm volatile("s_mov_b32 m0, %3\n\ts_nop 0\n\tbuffer_load_dword %0, %1, %2 offen lds" :: "v"(l < 3 ? l * 128 : OOB), "s"(mk_rsrc((const void*)pc, CODE_BYTES)), "s"((bi * 8 + w) * 384), "s"(L_ST) : "memory");
  }
  asm volatile("" :: "s"(p.x), "s"(p.wqkv), "s"(p.wo), "s"(p.w1), "s"(p.w3), "s"(p.w2), "s"(p.ws));
  if (PF0) dma16(L_ST, l * 16, mk_rsrc(w % 3 == 0 ? p.w1 : (w % 3 == 1 ? p.w3 : p.wqkv), D * F * 2), ((w / 3) * 256 + b) * 1024);
  const rsrc_t sws = mk_rsrc(p.ws, WS_BYTES);
  const int panel = (x >> 1) * 8 + (bi >> 2), cg2 = (x & 1) * 4 + (bi & 3);
  if (bi < 18) {
    const int j = xn * 18 + bi;
    ffn_wg(p, sws, xm * 8 + (j & 7), j >> 3, panel, cg2, w, l STAMP_PASS);
  } else {
    const int j = bi - 18, rt = xm * 7 + (j % 7), hg = xn * 2 + j / 7;
    att_wg(p, sws, rt, hg, panel, cg2, w, l STAMP_PASS);
  }
}

extern "C" void kernel_launch(void* const* d_in, const int* in_sizes, int n_in, void* d_out, int out_size, void* d_ws, size_t ws_size,
                              hipStream_t stream) {
  P p;
  p.x = (const u16*)d_in[0]; p.wqkv = (const u16*)d_in[1]; p.wo = (const u16*)d_in[2];
  p.w1 = (const u16*)d_in[4]; p.w3 = (const u16*)d_in[5]; p.w2 = (const u16*)d_in[6];
  p.out = (u16*)d_out; p.ws = (char*)d_ws;
  (void)hipFuncSetAttribute((const void*)fused, hipFuncAttributeMaxDynamicSharedMemorySize, LDS_BYTES);
  fused<<<256, 512, LDS_BYTES, stream>>>(p);
}
```
